```python
import torch

def _softmax(x, axis=-1):
    return torch.softmax(x, dim=axis)
def _gelu(x):
    return torch.nn.functional.gelu(x, approximate="tanh")
def _silu(x):
    return torch.nn.functional.silu(x)
def _sigmoid(x):
    return torch.sigmoid(x)
def _layernorm(x, axis=-1, eps=1e-5):
    m = x.mean(dim=axis, keepdim=True)
    v = x.var(dim=axis, keepdim=True, unbiased=False)
    return (x - m) / torch.sqrt(v + eps)
def _scatter_add(tgt, idx, src, axis=0):
    return tgt.clone().index_add_(axis, idx, src)
def _segment_sum(src, seg, n):
    out = torch.zeros((n,) + src.shape[1:], dtype=src.dtype)
    return out.index_add_(0, seg, src)
def _pad(x, pw):
    flat = tuple(v for pair in reversed(pw) for v in pair)
    return torch.nn.functional.pad(x, flat)

kernel_name = 'interleave_layers__linear_attn_block__mixer_channel_mlp__prompt'

def setup_inputs():

    _w = (lambda a: (lambda t: t.to(torch.bfloat16) if t.is_floating_point() else t)(torch.as_tensor(a)))
    def _A_setup():
        B, S, D = 1, 2048, 1024
        return {
            "x": _w((lambda *s: torch.randn(*s, dtype=torch.bfloat16) * 0.05)(B, S, D)),
            "wqkv": _w((lambda *s: torch.randn(*s, dtype=torch.bfloat16) * 0.05)(D, 3 * D)),
            "wo": _w((lambda *s: torch.randn(*s, dtype=torch.bfloat16) * 0.05)(D, D)),
        }
    _A_in = _A_setup()

    def _B_setup():
        B, S, D, F = 1, 2048, 1024, 3584
        return {
            "x": _w((lambda *s: torch.randn(*s, dtype=torch.bfloat16) * 0.05)(B, S, D)),
            "w1": _w((lambda *s: torch.randn(*s, dtype=torch.bfloat16) * 3.2)(D, F) * (D ** -0.5)),
            "b1": _w((lambda *s: torch.randn(*s, dtype=torch.bfloat16) * 0.4)(F)),
            "w2": _w((lambda *s: torch.randn(*s, dtype=torch.bfloat16) * 0.4)(F, D) * (F ** -0.5)),
            "b2": _w((lambda *s: torch.randn(*s, dtype=torch.bfloat16) * 0.4)(D)),
        }
    _B_in = _B_setup()

    return {
        "A_x": _A_in["x"],
        "A_wqkv": _A_in["wqkv"],
        "A_wo": _A_in["wo"],
        "B_x": _B_in["x"],
        "B_w1": _B_in["w1"],
        "B_b1": _B_in["b1"],
        "B_w2": _B_in["w2"],
        "B_b2": _B_in["b2"],
    }

def reference(A_x, A_wqkv, A_wo, B_x, B_w1, B_b1, B_w2, B_b2):
    def _A_ref(x, wqkv, wo):
        B, S, D = x.shape
        H = 32; Dh = D // H
        qkv = (lambda x, *d: x.permute(*d))((x @ wqkv).reshape(B, S, 3, H, Dh), 2, 0, 3, 1, 4)
        q, k, v = qkv[0], qkv[1], qkv[2]
        phi = lambda t: torch.where(t > 0, t + 1, torch.exp(t)) + 1e-6
        pq, pk = phi(q), phi(k)
        kv = torch.einsum("bhsd,bhse->bhde", pk, v)
        z = torch.einsum("bhsd->bhd", pk)
        num = torch.einsum("bhqd,bhde->bhqe", pq, kv)
        den = torch.einsum("bhqd,bhd->bhq", pq, z)[..., None] + 1e-6
        o = num / den
        return (lambda x, *d: x.permute(*d))(o, 0, 2, 1, 3).reshape(B, S, D) @ wo

    def _B_ref(x, w1, b1, w2, b2):
        y = _layernorm(x, axis=-1)
        h = _gelu(torch.einsum("bsd,df->bsf", y, w1) + b1)
        return torch.einsum("bsf,fd->bsd", h, w2) + b2

    x = A_x
    x = x + _A_ref(_layernorm(x, axis=-1), A_wqkv, A_wo)
    x = x + _B_ref(_layernorm(x, axis=-1), B_w1, B_b1, B_w2, B_b2)
    x = x + _A_ref(_layernorm(x, axis=-1), A_wqkv, A_wo)
    x = x + _B_ref(_layernorm(x, axis=-1), B_w1, B_b1, B_w2, B_b2)
    return x
```

```cpp
#include <hip/hip_runtime.h>
#include <stdint.h>

typedef unsigned short u16;
typedef __attribute__((ext_vector_type(8))) short bf16x8;
typedef __attribute__((ext_vector_type(4))) short bf16x4;
typedef __attribute__((ext_vector_type(4))) float f32x4;
typedef __attribute__((ext_vector_type(4))) unsigned short u16x4;
typedef __attribute__((ext_vector_type(8))) unsigned short u16x8;

constexpr int S_ = 2048, D_ = 1024, F_ = 3584, H_ = 32;

__device__ __forceinline__ float bf2f(u16 u) { return __uint_as_float(((unsigned)u) << 16); }
__device__ __forceinline__ u16 f2bf(float f) { return __builtin_bit_cast(u16, (__bf16)f); }
__device__ __forceinline__ float rbf(float f) { return bf2f(f2bf(f)); }

enum { EPI_QKV = 0, EPI_GELU = 1, EPI_PART = 2 };

template <int BN, int EPI>
__global__ __launch_bounds__(256) void gemm_k(const u16* __restrict__ A, const u16* __restrict__ B,
                                              const u16* __restrict__ bias, void* __restrict__ out,
                                              int lda, int N, int Kslice, int gn_log) {
  constexpr int NT = BN / 32;
  constexpr int BSTAGE = BN * 128;
  constexpr int STAGE = 16384 + BSTAGE;
  constexpr int NBI = BN / 8;
  constexpr int LPW = 4 + NBI / 4;
  __shared__ __attribute__((aligned(16))) char lds[3 * STAGE];

  const int tid = threadIdx.x, lane = tid & 63, w = tid >> 6;
  const int wm = w >> 1, wn = w & 1;
  const int bid = blockIdx.x, xcd = bid & 7, idx = bid >> 3;
  int tm, tn, ks;
  if (EPI == EPI_PART) {
    tm = (xcd >> 1) * 4 + (idx & 3);
    tn = (xcd & 1) * 4 + ((idx >> 2) & 3);
    ks = idx >> 4;
  } else {
    tm = (xcd >> 1) * 4 + (idx & 3);
    tn = (xcd & 1) * 8 + (idx >> 2);
    ks = 0;
  }
  const int m0 = tm * 128, n0 = tn * BN, k0 = ks * Kslice;

  const u16* aptr = A + (size_t)(m0 + w * 32 + (lane >> 3)) * lda + k0 + (((lane & 7) ^ (lane >> 3)) * 8);
  const u16* bptr[NBI / 4];
  int boff[NBI / 4];
#pragma unroll
  for (int i = 0; i < NBI / 4; ++i) {
    int j = w + i * 4;
    int blk = j * 4 + (lane >> 4);
    int kb = blk / (BN / 16), cb = blk % (BN / 16);
    int u = lane & 15, r = u >> 1, hf = u & 1;
    bptr[i] = B + (size_t)(k0 + kb * 8 + (r ^ ((kb & 1) * 4))) * N + n0 + cb * 16 + hf * 8;
    boff[i] = 16384 + j * 1024 + lane * 16;
  }
  const int aoff = w * 4096 + lane * 16;

  auto stage_load = [&](int kt, int slot) {
    char* base = lds + slot * STAGE;
#pragma unroll
    for (int i = 0; i < 4; ++i)
      __builtin_amdgcn_global_load_lds((const unsigned*)(aptr + (size_t)i * 8 * lda + kt * 64),
                                       (unsigned*)(base + aoff + i * 1024), 16, 0, 0);
#pragma unroll
    for (int i = 0; i < NBI / 4; ++i)
      __builtin_amdgcn_global_load_lds((const unsigned*)(bptr[i] + (size_t)kt * 64 * N),
                                       (unsigned*)(base + boff[i]), 16, 0, 0);
  };

  f32x4 acc[4][NT];
#pragma unroll
  for (int i = 0; i < 4; ++i)
#pragma unroll
    for (int j = 0; j < NT; ++j) acc[i][j] = (f32x4){0.f, 0.f, 0.f, 0.f};

  int ard[4];
#pragma unroll
  for (int mi = 0; mi < 4; ++mi) {
    int row = wm * 64 + mi * 16 + (lane & 15);
    ard[mi] = row * 128 + (((lane >> 4) ^ (row & 7)) * 16);
  }
  const int g = lane >> 4, q = (lane & 15) >> 2, p = lane & 3;
  const int brd0 = 16384 + (g * (BN / 16) + wn * NT) * 256 + ((q ^ ((g & 1) * 4)) * 32) + p * 8;

  const int hioff = (g & 1) ? -128 : 128;
  const int nk = Kslice / 64;
  stage_load(0, 0);
  stage_load(1, 1);
  int slot = 0;
  for (int kt = 0; kt < nk; ++kt) {
    if (kt + 1 < nk) asm volatile("s_waitcnt vmcnt(%0)" ::"n"(LPW) : "memory");
    else asm volatile("s_waitcnt vmcnt(0)" ::: "memory");
    __builtin_amdgcn_s_barrier();
    if (kt + 2 < nk) { int s2 = slot + 2; if (s2 >= 3) s2 -= 3; stage_load(kt + 2, s2); }
    const char* base = lds + slot * STAGE;
#pragma unroll
    for (int s = 0; s < 2; ++s) {
      bf16x8 af[4], bfr[NT];
#pragma unroll
      for (int mi = 0; mi < 4; ++mi) af[mi] = *(const bf16x8*)(base + (ard[mi] ^ (s * 64)));
#pragma unroll
      for (int ni = 0; ni < NT; ++ni) {
        typedef __attribute__((address_space(3))) bf16x4 lds_v4;
        const char* pb = base + brd0 + s * 4 * (BN / 16) * 256 + ni * 256;
        bf16x4 lo = __builtin_amdgcn_ds_read_tr16_b64_v4i16((lds_v4*)(pb));
        bf16x4 hi = __builtin_amdgcn_ds_read_tr16_b64_v4i16((lds_v4*)(pb + hioff));
        bfr[ni] = __builtin_shufflevector(lo, hi, 0, 1, 2, 3, 4, 5, 6, 7);
      }
#pragma unroll
      for (int mi = 0; mi < 4; ++mi)
#pragma unroll
        for (int ni = 0; ni < NT; ++ni)
          acc[mi][ni] = __builtin_amdgcn_mfma_f32_16x16x32_bf16(bfr[ni], af[mi], acc[mi][ni], 0, 0, 0);
    }
    slot = slot + 1; if (slot >= 3) slot = 0;
  }

#pragma unroll
  for (int mi = 0; mi < 4; ++mi) {
    const int row = m0 + wm * 64 + mi * 16 + (lane & 15);
#pragma unroll
    for (int ni = 0; ni < NT; ++ni) {
      const int col = n0 + wn * (BN / 2) + ni * 16 + (lane >> 4) * 4;
      if (EPI == EPI_PART) {
        float* o = (float*)out + (size_t)ks * S_ * N + (size_t)row * N + col;
        *(f32x4*)o = acc[mi][ni];
      } else if (EPI == EPI_QKV) {
        u16x4 r;
#pragma unroll
        for (int j = 0; j < 4; ++j) {
          float t = rbf(acc[mi][ni][j]);
          if (col < 2 * D_) {
            float f = t > 0.f ? rbf(t + 1.f) : rbf(__expf(t));
            t = rbf(f + 1e-6f);
          }
          r[j] = f2bf(t);
        }
        *(u16x4*)((u16*)out + (size_t)row * N + col) = r;
      } else {
        u16x4 bb = *(const u16x4*)(bias + col);
        u16x4 r;
#pragma unroll
        for (int j = 0; j < 4; ++j) {
          float t = rbf(rbf(acc[mi][ni][j]) + bf2f(bb[j]));
          float u = 0.7978845608028654f * (t + 0.044715f * t * t * t);
          float th = 1.f - 2.f / (__expf(2.f * u) + 1.f);
          r[j] = f2bf(0.5f * t * (1.f + th));
        }
        *(u16x4*)((u16*)out + (size_t)row * N + col) = r;
      }
    }
  }
}

__device__ __forceinline__ float wave_sum(float v) {
#pragma unroll
  for (int o = 32; o >= 1; o >>= 1) v += __shfl_xor(v, o, 64);
  return v;
}

__global__ __launch_bounds__(256) void row_k(const u16* __restrict__ xprev, const float* __restrict__ part,
                                             const u16* __restrict__ bias, u16* __restrict__ xnew,
                                             u16* __restrict__ xn, int mode) {
  const int lane = threadIdx.x & 63, row = blockIdx.x * 4 + (threadIdx.x >> 6);
  float v[16];
  u16x8 xa = *(const u16x8*)(xprev + (size_t)row * D_ + lane * 8);
  u16x8 xb = *(const u16x8*)(xprev + (size_t)row * D_ + 512 + lane * 8);
#pragma unroll
  for (int j = 0; j < 8; ++j) { v[j] = bf2f(xa[j]); v[8 + j] = bf2f(xb[j]); }
  if (mode & 1) {
#pragma unroll
    for (int hh = 0; hh < 2; ++hh) {
      const float* p0 = part + (size_t)row * D_ + hh * 512 + lane * 8;
      const float* p1 = p0 + (size_t)S_ * D_;
      f32x4 a0 = *(const f32x4*)p0, a1 = *(const f32x4*)(p0 + 4);
      f32x4 b0 = *(const f32x4*)p1, b1 = *(const f32x4*)(p1 + 4);
      float bsv[8];
      if (bias) {
        u16x8 bb = *(const u16x8*)(bias + hh * 512 + lane * 8);
#pragma unroll
        for (int j = 0; j < 8; ++j) bsv[j] = bf2f(bb[j]);
      }
#pragma unroll
      for (int j = 0; j < 8; ++j) {
        float s = (j < 4 ? a0[j] + b0[j] : a1[j - 4] + b1[j - 4]);
        s = rbf(s);
        if (bias) s = rbf(s + bsv[j]);
        v[hh * 8 + j] = rbf(v[hh * 8 + j] + s);
      }
    }
    u16x8 oa, ob;
#pragma unroll
    for (int j = 0; j < 8; ++j) { oa[j] = f2bf(v[j]); ob[j] = f2bf(v[8 + j]); }
    *(u16x8*)(xnew + (size_t)row * D_ + lane * 8) = oa;
    *(u16x8*)(xnew + (size_t)row * D_ + 512 + lane * 8) = ob;
  }
  if (mode & 4) return;
  const int npass = (mode & 2) ? 2 : 1;
  for (int ps = 0; ps < npass; ++ps) {
    float s = 0.f;
#pragma unroll
    for (int j = 0; j < 16; ++j) s += v[j];
    float mean = wave_sum(s) * (1.f / D_);
    float q = 0.f;
#pragma unroll
    for (int j = 0; j < 16; ++j) { float d = v[j] - mean; q += d * d; }
    float var = wave_sum(q) * (1.f / D_);
    float rs = rsqrtf(var + 1e-5f);
#pragma unroll
    for (int j = 0; j < 16; ++j) v[j] = rbf((v[j] - mean) * rs);
  }
  u16x8 oa, ob;
#pragma unroll
  for (int j = 0; j < 8; ++j) { oa[j] = f2bf(v[j]); ob[j] = f2bf(v[8 + j]); }
  *(u16x8*)(xn + (size_t)row * D_ + lane * 8) = oa;
  *(u16x8*)(xn + (size_t)row * D_ + 512 + lane * 8) = ob;
}

__global__ __launch_bounds__(256) void kv_k(const u16* __restrict__ qkv, float* __restrict__ kvp) {
  __shared__ float pk_s[256][33];
  __shared__ float v_s[256][36];
  const int h = blockIdx.x, c = blockIdx.y, t = threadIdx.x;
  {
    const u16* rowp = qkv + (size_t)(c * 256 + t) * 3072;
#pragma unroll
    for (int i = 0; i < 4; ++i) {
      u16x8 a = *(const u16x8*)(rowp + 1024 + h * 32 + i * 8);
      u16x8 b = *(const u16x8*)(rowp + 2048 + h * 32 + i * 8);
#pragma unroll
      for (int j = 0; j < 8; ++j) { pk_s[t][i * 8 + j] = bf2f(a[j]); v_s[t][i * 8 + j] = bf2f(b[j]); }
    }
  }
  __syncthreads();
  const int d = t >> 3, e0 = (t & 7) * 4;
  float a0 = 0.f, a1 = 0.f, a2 = 0.f, a3 = 0.f, z = 0.f;
  for (int r = 0; r < 256; ++r) {
    float kk = pk_s[r][d];
    f32x4 vv = *(const f32x4*)&v_s[r][e0];
    a0 += kk * vv[0]; a1 += kk * vv[1]; a2 += kk * vv[2]; a3 += kk * vv[3];
    z += kk;
  }
  float* o = kvp + ((size_t)c * H_ + h) * 1056;
  *(f32x4*)(o + d * 32 + e0) = (f32x4){a0, a1, a2, a3};
  if ((t & 7) == 0) o[1024 + d] = z;
}

__global__ __launch_bounds__(256) void ap_k(const u16* __restrict__ qkv, const float* __restrict__ kvp,
                                            u16* __restrict__ o) {
  __shared__ float kv_s[1056];
  const int h = blockIdx.x, c = blockIdx.y, t = threadIdx.x;
  for (int i = t; i < 1056; i += 256) {
    float s = 0.f;
#pragma unroll
    for (int cc = 0; cc < 8; ++cc) s += kvp[((size_t)cc * H_ + h) * 1056 + i];
    kv_s[i] = rbf(s);
  }
  __syncthreads();
  const int row = c * 256 + t;
  float pq[32];
#pragma unroll
  for (int i = 0; i < 4; ++i) {
    u16x8 a = *(const u16x8*)(qkv + (size_t)row * 3072 + h * 32 + i * 8);
#pragma unroll
    for (int j = 0; j < 8; ++j) pq[i * 8 + j] = bf2f(a[j]);
  }
  float den = 0.f;
#pragma unroll
  for (int d = 0; d < 32; ++d) den += pq[d] * kv_s[1024 + d];
  den = rbf(rbf(den) + 1e-6f);
  float inv = 1.f / den;
#pragma unroll
  for (int i = 0; i < 4; ++i) {
    float num[8];
#pragma unroll
    for (int j = 0; j < 8; ++j) num[j] = 0.f;
#pragma unroll
    for (int d = 0; d < 32; ++d) {
#pragma unroll
      for (int j = 0; j < 8; ++j) num[j] += pq[d] * kv_s[d * 32 + i * 8 + j];
    }
    u16x8 r;
#pragma unroll
    for (int j = 0; j < 8; ++j) r[j] = f2bf(rbf(num[j]) * inv);
    *(u16x8*)(o + (size_t)row * D_ + h * 32 + i * 8) = r;
  }
}

extern "C" void kernel_launch(void* const* d_in, const int* in_sizes, int n_in, void* d_out, int out_size,
                              void* d_ws, size_t ws_size, hipStream_t stream) {
  const u16* x0 = (const u16*)d_in[0];
  const u16* wqkv = (const u16*)d_in[1];
  const u16* wo = (const u16*)d_in[2];
  const u16* w1 = (const u16*)d_in[4];
  const u16* b1 = (const u16*)d_in[5];
  const u16* w2 = (const u16*)d_in[6];
  const u16* b2 = (const u16*)d_in[7];
  char* ws = (char*)d_ws;
  u16* xa = (u16*)(ws);
  u16* xb = (u16*)(ws + (4u << 20));
  u16* xn = (u16*)(ws + (8u << 20));
  u16* qkv = (u16*)(ws + (12u << 20));
  u16* ob = (u16*)(ws + (24u << 20));
  u16* hb = (u16*)(ws + (28u << 20));
  float* part = (float*)(ws + (44u << 20));
  float* kvp = (float*)(ws + (60u << 20));

  const u16* xcur = x0;
  u16* xout[4] = {xa, xb, xa, (u16*)d_out};
#define KSTOP 1000
  int kc = 0;
#define RUN(...) do { if (kc++ < KSTOP) { __VA_ARGS__; } } while (0)
  RUN(row_k<<<512, 256, 0, stream>>>(x0, nullptr, nullptr, nullptr, xn, 0));
  for (int l = 0; l < 2; ++l) {
    RUN(gemm_k<192, EPI_QKV><<<256, 256, 0, stream>>>(xn, wqkv, nullptr, qkv, D_, 3 * D_, D_, 0));
    RUN(kv_k<<<dim3(32, 8), 256, 0, stream>>>(qkv, kvp));
    RUN(ap_k<<<dim3(32, 8), 256, 0, stream>>>(qkv, kvp, ob));
    RUN(gemm_k<128, EPI_PART><<<256, 256, 0, stream>>>(ob, wo, nullptr, part, D_, D_, D_ / 2, 0));
    RUN(row_k<<<512, 256, 0, stream>>>(xcur, part, nullptr, xout[2 * l], xn, 1 | 2));
    xcur = xout[2 * l];
    RUN(gemm_k<224, EPI_GELU><<<256, 256, 0, stream>>>(xn, w1, b1, hb, D_, F_, D_, 0));
    RUN(gemm_k<128, EPI_PART><<<256, 256, 0, stream>>>(hb, w2, nullptr, part, F_, D_, F_ / 2, 0));
    RUN(row_k<<<512, 256, 0, stream>>>(xcur, part, b2, xout[2 * l + 1], xn, l == 0 ? 1 : (1 | 4)));
    xcur = xout[2 * l + 1];
  }
}
```

```cpp
#include <hip/hip_runtime.h>
#include <stdint.h>

typedef unsigned short u16;
typedef __attribute__((ext_vector_type(8))) short bf16x8;
typedef __attribute__((ext_vector_type(4))) short bf16x4;
typedef __attribute__((ext_vector_type(4))) float f32x4;
typedef __attribute__((ext_vector_type(4))) unsigned short u16x4;
typedef __attribute__((ext_vector_type(8))) unsigned short u16x8;

constexpr int S_ = 2048, D_ = 1024, F_ = 3584, H_ = 32;

__device__ __forceinline__ float bf2f(u16 u) { return __uint_as_float(((unsigned)u) << 16); }
__device__ __forceinline__ u16 f2bf(float f) { return __builtin_bit_cast(u16, (__bf16)f); }
__device__ __forceinline__ float rbf(float f) { return bf2f(f2bf(f)); }

__device__ __forceinline__ void glds16(const void* gsrc, unsigned lds_dst) {
  unsigned keep;
  asm volatile("s_mov_b32 %0, m0\n\ts_mov_b32 m0, %2\n\ts_nop 0\n\tglobal_load_lds_dwordx4 %1, off\n\ts_mov_b32 m0, %0"
               : "=&s"(keep) : "v"(gsrc), "s"(lds_dst) : "memory");
}

enum { EPI_QKV = 0, EPI_GELU = 1, EPI_PART = 2 };

template <int BN, int EPI>
__global__ __launch_bounds__(256) void gemm_k(const u16* __restrict__ A, const u16* __restrict__ B,
                                              const u16* __restrict__ bias, void* __restrict__ out,
                                              int lda, int N, int Kslice, int gn_log) {
  constexpr int NT = BN / 32;
  constexpr int BSTAGE = BN * 128;
  constexpr int STAGE = 16384 + BSTAGE;
  constexpr int NBI = BN / 8;
  constexpr int LPW = 4 + NBI / 4;
  constexpr int NSLOT = (BN <= 128) ? 5 : (BN <= 192 ? 4 : 3);
  __shared__ __attribute__((aligned(16))) char lds[NSLOT * STAGE];

  const int tid = threadIdx.x, lane = tid & 63, w = tid >> 6;
  const int wm = w >> 1, wn = w & 1;
  const int bid = blockIdx.x, xcd = bid & 7, idx = bid >> 3;
  int tm, tn, ks;
  if (EPI == EPI_PART) {
    tm = (xcd >> 1) * 4 + (idx & 3);
    tn = (xcd & 1) * 4 + ((idx >> 2) & 3);
    ks = idx >> 4;
  } else {
    tm = (xcd >> 1) * 4 + (idx & 3);
    tn = (xcd & 1) * 8 + (idx >> 2);
    ks = 0;
  }
  const int m0 = tm * 128, n0 = tn * BN, k0 = ks * Kslice;

  const u16* aptr = A + (size_t)(m0 + w * 32 + (lane >> 3)) * lda + k0 + (((lane & 7) ^ (lane >> 3)) * 8);
  const u16* bptr[NBI / 4];
  int boff[NBI / 4];
#pragma unroll
  for (int i = 0; i < NBI / 4; ++i) {
    int j = w + i * 4;
    int blk = j * 4 + (lane >> 4);
    int kb = blk / (BN / 16), cb = blk % (BN / 16);
    int u = lane & 15, r = u >> 1, hf = u & 1;
    bptr[i] = B + (size_t)(k0 + kb * 8 + (r ^ ((kb & 1) * 4))) * N + n0 + cb * 16 + hf * 8;
    boff[i] = 16384 + j * 1024 + lane * 16;
  }
  const int aoff = w * 4096 + lane * 16;

  const unsigned ldsbase = __builtin_amdgcn_readfirstlane((unsigned)(size_t)lds);
  const unsigned aoffu = __builtin_amdgcn_readfirstlane(w * 4096);
  unsigned boffu[NBI / 4];
#pragma unroll
  for (int i = 0; i < NBI / 4; ++i) boffu[i] = __builtin_amdgcn_readfirstlane(16384 + (w + i * 4) * 1024);
  auto stage_load = [&](int kt, int slot) {
    unsigned base = ldsbase + slot * STAGE;
#pragma unroll
    for (int i = 0; i < 4; ++i) glds16(aptr + (size_t)i * 8 * lda + kt * 64, base + aoffu + i * 1024);
#pragma unroll
    for (int i = 0; i < NBI / 4; ++i) glds16(bptr[i] + (size_t)kt * 64 * N, base + boffu[i]);
  };

  f32x4 acc[4][NT];
#pragma unroll
  for (int i = 0; i < 4; ++i)
#pragma unroll
    for (int j = 0; j < NT; ++j) acc[i][j] = (f32x4){0.f, 0.f, 0.f, 0.f};

  int ard[4];
#pragma unroll
  for (int mi = 0; mi < 4; ++mi) {
    int row = wm * 64 + mi * 16 + (lane & 15);
    ard[mi] = row * 128 + (((lane >> 4) ^ (row & 7)) * 16);
  }
  const int g = lane >> 4, q = (lane & 15) >> 2, p = lane & 3;
  const int brd0 = 16384 + (g * (BN / 16) + wn * NT) * 256 + ((q ^ ((g & 1) * 4)) * 32) + p * 8;

  const int hioff = (g & 1) ? -128 : 128;
  const int nk = Kslice / 64;
  bf16x8 fa0[4], fb0[NT], fa1[4], fb1[NT];
  typedef __attribute__((address_space(3))) bf16x4 lds_v4;
#define READ_OP(FA, FB, BASE, SUB, I)                                                              \
  do {                                                                                             \
    if ((I) < 4) {                                                                                 \
      FA[(I) < 4 ? (I) : 0] = *(const bf16x8*)((BASE) + (ard[(I) < 4 ? (I) : 0] ^ ((SUB) * 64)));  \
    } else if ((I) < 4 + NT) {                                                                     \
      const int ni_ = (I) - 4 < NT ? (I) - 4 : 0;                                                  \
      const char* pb = (BASE) + brd0 + (SUB) * 4 * (BN / 16) * 256 + ni_ * 256;                    \
      bf16x4 lo = __builtin_amdgcn_ds_read_tr16_b64_v4i16((lds_v4*)(pb));                          \
      bf16x4 hi = __builtin_amdgcn_ds_read_tr16_b64_v4i16((lds_v4*)(pb + hioff));                  \
      FB[ni_] = __builtin_shufflevector(lo, hi, 0, 1, 2, 3, 4, 5, 6, 7);                           \
    }                                                                                              \
  } while (0)
#define MFMA_OP(FA, FB, I)                                                                         \
  asm("v_mfma_f32_16x16x32_bf16 %0, %1, %2, %0" : "+v"(acc[(I) / NT][(I) % NT]) : "v"(FB[(I) % NT]), "v"(FA[(I) / NT]))

#pragma unroll
  for (int i = 0; i < NSLOT; ++i) stage_load(i, i);
  asm volatile("s_waitcnt vmcnt(%0)" ::"n"((NSLOT - 1) * LPW) : "memory");
  __builtin_amdgcn_s_barrier();
#pragma unroll
  for (int i = 0; i < 4 + NT; ++i) READ_OP(fa0, fb0, lds, 0, i);
  int slot = 0;
  for (int kt = 0; kt < nk; ++kt) {
    const char* base = lds + slot * STAGE;
#pragma unroll
    for (int i = 0; i < 4 * NT; ++i) {
      MFMA_OP(fa0, fb0, i);
      READ_OP(fa1, fb1, base, 1, i);
      __builtin_amdgcn_sched_barrier(0);
    }
    asm volatile("s_waitcnt lgkmcnt(0)" ::: "memory");
    int nslot = slot + 1; if (nslot >= NSLOT) nslot = 0;
    asm volatile("s_waitcnt vmcnt(%0)" ::"n"((NSLOT - 2) * LPW) : "memory");
    __builtin_amdgcn_s_barrier();
    const char* nbase = lds + nslot * STAGE;
    const int ktl = min(kt + NSLOT, nk - 1);
    const unsigned sbase = ldsbase + slot * STAGE;
#pragma unroll
    for (int i = 0; i < 4 * NT; ++i) {
      MFMA_OP(fa1, fb1, i);
      READ_OP(fa0, fb0, nbase, 0, i);
      if (i >= 4 * NT - LPW) {
        const int li = i - (4 * NT - LPW);
        if (li < 4) glds16(aptr + (size_t)li * 8 * lda + ktl * 64, sbase + aoffu + li * 1024);
        else glds16(bptr[li - 4 < NBI / 4 ? li - 4 : 0] + (size_t)ktl * 64 * N, sbase + boffu[li - 4 < NBI / 4 ? li - 4 : 0]);
      }
      __builtin_amdgcn_sched_barrier(0);
    }
    slot = nslot;
  }
  asm volatile("s_waitcnt vmcnt(0)" ::: "memory");
  __builtin_amdgcn_s_barrier();

  {
    constexpr int RS = BN * 2 + 16;
#pragma unroll
    for (int mi = 0; mi < 4; ++mi) {
#pragma unroll
      for (int ni = 0; ni < NT; ++ni) {
        const int col = n0 + wn * (BN / 2) + ni * 16 + (lane >> 4) * 4;
        float r[4];
        if (EPI == EPI_QKV) {
          const bool isqk = col < 2 * D_;
#pragma unroll
          for (int j = 0; j < 4; ++j) {
            float t = acc[mi][ni][j];
            float f = (t > 0.f ? t + 1.f : __expf(t)) + 1e-6f;
            r[j] = isqk ? f : t;
          }
        } else if (EPI == EPI_GELU) {
          u16x4 bb = *(const u16x4*)(bias + col);
#pragma unroll
          for (int j = 0; j < 4; ++j) {
            float t = acc[mi][ni][j] + bf2f(bb[j]);
            float u2 = 1.5957691216057308f * (t + 0.044715f * t * t * t);
            r[j] = t - t / (__expf(u2) + 1.f);
          }
        } else {
#pragma unroll
          for (int j = 0; j < 4; ++j) r[j] = acc[mi][ni][j];
        }
        u16x4 rr;
#pragma unroll
        for (int j = 0; j < 4; ++j) rr[j] = f2bf(r[j]);
        *(u16x4*)(lds + (wm * 64 + mi * 16 + (lane & 15)) * RS + (wn * (BN / 2) + ni * 16 + (lane >> 4) * 4) * 2) = rr;
      }
    }
    __syncthreads();
    constexpr int CPR = BN * 2 / 16;
    u16* ob = (u16*)out + (EPI == EPI_PART ? (size_t)ks * S_ * N : 0);
    for (int c = tid; c < 128 * CPR; c += 256) {
      int r = c / CPR, cc = c % CPR;
      u16x8 vv = *(const u16x8*)(lds + r * RS + cc * 16);
      *(u16x8*)(ob + (size_t)(m0 + r) * N + n0 + cc * 8) = vv;
    }
  }
}

__device__ __forceinline__ float wave_sum(float v) {
#pragma unroll
  for (int o = 32; o >= 1; o >>= 1) v += __shfl_xor(v, o, 64);
  return v;
}

__global__ __launch_bounds__(256) void row_k(const u16* __restrict__ xprev, const u16* __restrict__ part,
                                             const u16* __restrict__ bias, u16* __restrict__ xnew,
                                             u16* __restrict__ xn, int mode) {
  const int lane = threadIdx.x & 63, row = blockIdx.x * 4 + (threadIdx.x >> 6);
  float v[16];
  u16x8 xa = *(const u16x8*)(xprev + (size_t)row * D_ + lane * 8);
  u16x8 xb = *(const u16x8*)(xprev + (size_t)row * D_ + 512 + lane * 8);
#pragma unroll
  for (int j = 0; j < 8; ++j) { v[j] = bf2f(xa[j]); v[8 + j] = bf2f(xb[j]); }
  if (mode & 1) {
#pragma unroll
    for (int hh = 0; hh < 2; ++hh) {
      const u16* p0 = part + (size_t)row * D_ + hh * 512 + lane * 8;
      u16x8 pa = *(const u16x8*)p0, pb = *(const u16x8*)(p0 + (size_t)S_ * D_);
      float bsv[8];
      if (bias) {
        u16x8 bb = *(const u16x8*)(bias + hh * 512 + lane * 8);
#pragma unroll
        for (int j = 0; j < 8; ++j) bsv[j] = bf2f(bb[j]);
      }
#pragma unroll
      for (int j = 0; j < 8; ++j) {
        float s = bf2f(pa[j]) + bf2f(pb[j]);
        s = rbf(s);
        if (bias) s = rbf(s + bsv[j]);
        v[hh * 8 + j] = rbf(v[hh * 8 + j] + s);
      }
    }
    u16x8 oa, ob;
#pragma unroll
    for (int j = 0; j < 8; ++j) { oa[j] = f2bf(v[j]); ob[j] = f2bf(v[8 + j]); }
    *(u16x8*)(xnew + (size_t)row * D_ + lane * 8) = oa;
    *(u16x8*)(xnew + (size_t)row * D_ + 512 + lane * 8) = ob;
  }
  if (mode & 4) return;
  const int npass = (mode & 2) ? 2 : 1;
  for (int ps = 0; ps < npass; ++ps) {
    float s = 0.f;
#pragma unroll
    for (int j = 0; j < 16; ++j) s += v[j];
    float mean = wave_sum(s) * (1.f / D_);
    float q = 0.f;
#pragma unroll
    for (int j = 0; j < 16; ++j) { float d = v[j] - mean; q += d * d; }
    float var = wave_sum(q) * (1.f / D_);
    float rs = rsqrtf(var + 1e-5f);
#pragma unroll
    for (int j = 0; j < 16; ++j) v[j] = rbf((v[j] - mean) * rs);
  }
  u16x8 oa, ob;
#pragma unroll
  for (int j = 0; j < 8; ++j) { oa[j] = f2bf(v[j]); ob[j] = f2bf(v[8 + j]); }
  *(u16x8*)(xn + (size_t)row * D_ + lane * 8) = oa;
  *(u16x8*)(xn + (size_t)row * D_ + 512 + lane * 8) = ob;
}

__global__ __launch_bounds__(256) void kv_k(const u16* __restrict__ qkv, float* __restrict__ kvp) {
  __shared__ float pk_s[128][33];
  __shared__ __attribute__((aligned(16))) float v_s[128][36];
  const int h = blockIdx.x, c = blockIdx.y, t = threadIdx.x;
  {
    const int r = t >> 1, hf = t & 1;
    const u16* rowp = qkv + (size_t)(c * 128 + r) * 3072 + h * 32 + hf * 16;
#pragma unroll
    for (int i = 0; i < 2; ++i) {
      u16x8 a = *(const u16x8*)(rowp + 1024 + i * 8);
      u16x8 b = *(const u16x8*)(rowp + 2048 + i * 8);
#pragma unroll
      for (int j = 0; j < 8; ++j) { pk_s[r][hf * 16 + i * 8 + j] = bf2f(a[j]); v_s[r][hf * 16 + i * 8 + j] = bf2f(b[j]); }
    }
  }
  __syncthreads();
  const int d = t >> 3, e0 = (t & 7) * 4;
  float a0 = 0.f, a1 = 0.f, a2 = 0.f, a3 = 0.f, z = 0.f;
#pragma unroll 8
  for (int r = 0; r < 128; ++r) {
    float kk = pk_s[r][d];
    f32x4 vv = *(const f32x4*)&v_s[r][e0];
    a0 += kk * vv[0]; a1 += kk * vv[1]; a2 += kk * vv[2]; a3 += kk * vv[3];
    z += kk;
  }
  float* o = kvp + ((size_t)c * H_ + h) * 1056;
  *(f32x4*)(o + d * 32 + e0) = (f32x4){a0, a1, a2, a3};
  if ((t & 7) == 0) o[1024 + d] = z;
}

__global__ __launch_bounds__(256) void ap_k(const u16* __restrict__ qkv, const float* __restrict__ kvp,
                                            u16* __restrict__ o) {
  __shared__ __attribute__((aligned(16))) float kv_s[1056];
  const int h = blockIdx.x, c = blockIdx.y, t = threadIdx.x;
  for (int i = t; i < 1056; i += 256) {
    float s = 0.f;
#pragma unroll
    for (int cc = 0; cc < 16; ++cc) s += kvp[((size_t)cc * H_ + h) * 1056 + i];
    kv_s[i] = rbf(s);
  }
  __syncthreads();
  const int row = c * 128 + (t >> 1), hf = t & 1;
  float pq[32];
#pragma unroll
  for (int i = 0; i < 4; ++i) {
    u16x8 a = *(const u16x8*)(qkv + (size_t)row * 3072 + h * 32 + i * 8);
#pragma unroll
    for (int j = 0; j < 8; ++j) pq[i * 8 + j] = bf2f(a[j]);
  }
  float den = 0.f;
#pragma unroll
  for (int d = 0; d < 32; ++d) den += pq[d] * kv_s[1024 + d];
  den = rbf(rbf(den) + 1e-6f);
  float inv = 1.f / den;
#pragma unroll
  for (int i = 0; i < 2; ++i) {
    float num[8];
#pragma unroll
    for (int j = 0; j < 8; ++j) num[j] = 0.f;
#pragma unroll
    for (int d = 0; d < 32; ++d) {
      f32x4 k0 = *(const f32x4*)&kv_s[d * 32 + hf * 16 + i * 8], k1 = *(const f32x4*)&kv_s[d * 32 + hf * 16 + i * 8 + 4];
#pragma unroll
      for (int j = 0; j < 4; ++j) { num[j] += pq[d] * k0[j]; num[4 + j] += pq[d] * k1[j]; }
    }
    u16x8 r;
#pragma unroll
    for (int j = 0; j < 8; ++j) r[j] = f2bf(rbf(num[j]) * inv);
    *(u16x8*)(o + (size_t)row * D_ + h * 32 + hf * 16 + i * 8) = r;
  }
}

extern "C" void kernel_launch(void* const* d_in, const int* in_sizes, int n_in, void* d_out, int out_size,
                              void* d_ws, size_t ws_size, hipStream_t stream) {
  const u16* x0 = (const u16*)d_in[0];
  const u16* wqkv = (const u16*)d_in[1];
  const u16* wo = (const u16*)d_in[2];
  const u16* w1 = (const u16*)d_in[4];
  const u16* b1 = (const u16*)d_in[5];
  const u16* w2 = (const u16*)d_in[6];
  const u16* b2 = (const u16*)d_in[7];
  char* ws = (char*)d_ws;
  u16* xa = (u16*)(ws);
  u16* xb = (u16*)(ws + (4u << 20));
  u16* xn = (u16*)(ws + (8u << 20));
  u16* qkv = (u16*)(ws + (12u << 20));
  u16* ob = (u16*)(ws + (24u << 20));
  u16* hb = (u16*)(ws + (28u << 20));
  u16* part = (u16*)(ws + (44u << 20));
  float* kvp = (float*)(ws + (60u << 20));

  const u16* xcur = x0;
  u16* xout[4] = {xa, xb, xa, (u16*)d_out};
#define KSTOP 1000
  int kc = 0;
#define RUN(...) do { if (kc++ < KSTOP) { __VA_ARGS__; } } while (0)
  RUN(row_k<<<512, 256, 0, stream>>>(x0, nullptr, nullptr, nullptr, xn, 0));
  for (int l = 0; l < 2; ++l) {
    RUN(gemm_k<192, EPI_QKV><<<256, 256, 0, stream>>>(xn, wqkv, nullptr, qkv, D_, 3 * D_, D_, 0));
    RUN(kv_k<<<dim3(32, 16), 256, 0, stream>>>(qkv, kvp));
    RUN(ap_k<<<dim3(32, 16), 256, 0, stream>>>(qkv, kvp, ob));
    RUN(gemm_k<128, EPI_PART><<<256, 256, 0, stream>>>(ob, wo, nullptr, part, D_, D_, D_ / 2, 0));
    RUN(row_k<<<512, 256, 0, stream>>>(xcur, part, nullptr, xout[2 * l], xn, 1 | 2));
    xcur = xout[2 * l];
    RUN(gemm_k<224, EPI_GELU><<<256, 256, 0, stream>>>(xn, w1, b1, hb, D_, F_, D_, 0));
    RUN(gemm_k<128, EPI_PART><<<256, 256, 0, stream>>>(hb, w2, nullptr, part, F_, D_, F_ / 2, 0));
    RUN(row_k<<<512, 256, 0, stream>>>(xcur, part, b2, xout[2 * l + 1], xn, l == 0 ? 1 : (1 | 4)));
    xcur = xout[2 * l + 1];
  }
}
```
